```python
import torch

def _softmax(x, axis=-1):
    return torch.softmax(x, dim=axis)
def _gelu(x):
    return torch.nn.functional.gelu(x, approximate="tanh")
def _silu(x):
    return torch.nn.functional.silu(x)
def _sigmoid(x):
    return torch.sigmoid(x)
def _layernorm(x, axis=-1, eps=1e-5):
    m = x.mean(dim=axis, keepdim=True)
    v = x.var(dim=axis, keepdim=True, unbiased=False)
    return (x - m) / torch.sqrt(v + eps)
def _scatter_add(tgt, idx, src, axis=0):
    return tgt.clone().index_add_(axis, idx, src)
def _segment_sum(src, seg, n):
    out = torch.zeros((n,) + src.shape[1:], dtype=src.dtype)
    return out.index_add_(0, seg, src)
def _pad(x, pw):
    flat = tuple(v for pair in reversed(pw) for v in pair)
    return torch.nn.functional.pad(x, flat)

kernel_name = 'standalone__paged_block_table_kv_radixattention__decode'

def setup_inputs():

    _w = (lambda a: (lambda t: t.to(torch.bfloat16) if t.is_floating_point() else t)(torch.as_tensor(a)))
    B, H, Hkv, Dh = 1, 32, 16, 128
    Lk = 8192
    bs = 16
    nb = max(2, Lk // bs)
    P = max(1, nb // 2)
    n_phys = P + B * (nb - P)
    shared = torch.arange(P, dtype=torch.int64)
    priv = P + torch.arange(B * (nb - P), dtype=torch.int64).reshape(B, nb - P)
    bt = torch.cat([torch.tile(shared[None, :], (B, 1)), priv], dim=1)
    ctx = nb * bs - (torch.arange(1, B + 1, dtype=torch.int64) * 2654435761) % max(1, (nb * bs - P * bs) // 2)
    return {
        "q": _w((lambda *s: torch.randn(*s, dtype=torch.bfloat16) * 0.8)(B, H, Dh)),
        "k_cache": _w((lambda *s: torch.randn(*s, dtype=torch.bfloat16) * 0.8)(n_phys, bs, Hkv, Dh)),
        "v_cache": _w((lambda *s: torch.randn(*s, dtype=torch.bfloat16) * 0.05)(n_phys, bs, Hkv, Dh)),
        "block_table": _w(bt.to(torch.int64)),
        "ctx_lens": _w(torch.as_tensor(ctx).to(torch.int64)),
    }

def reference(q, k_cache, v_cache, block_table, ctx_lens):
    B, H, Dh = q.shape
    n_phys, bs, Hkv, _ = k_cache.shape
    nb = block_table.shape[1]
    Lk = nb * bs
    rep = H // Hkv
    k = k_cache[block_table].reshape(B, Lk, Hkv, Dh)
    v = v_cache[block_table].reshape(B, Lk, Hkv, Dh)
    k = (lambda x, n, axis: torch.repeat_interleave(x, n, dim=axis))(k, rep, axis=2)
    v = (lambda x, n, axis: torch.repeat_interleave(x, n, dim=axis))(v, rep, axis=2)
    scores = torch.einsum("bhd,bkhd->bhk", q, k) * (Dh ** -0.5)
    pos = torch.arange(Lk)
    valid = pos[None, :] < ctx_lens[:, None]
    scores = torch.where(valid[:, None, :], scores, -1e9)
    p = _softmax(scores, axis=-1)
    return torch.einsum("bhk,bkhd->bhd", p, v)
```

```cpp
#include <hip/hip_runtime.h>
#include <cstdint>

constexpr int WAVES = 8, NSPLIT = 16, WIN = 3;
constexpr int D = 128, PAGE = 16, HQ = 32, HKV = 16, NB = 512, NPHYS = 512, LK = NB * PAGE;
constexpr int NL = (NPHYS * 4) / (NSPLIT * WAVES);
constexpr int NG = NL / 8;
constexpr int PSTEP = NSPLIT * WAVES / 4;
constexpr int TOK_BYTES = HKV * D * 2, PAGE_BYTES = PAGE * TOK_BYTES;
constexpr unsigned KV_BYTES = (unsigned)NPHYS * PAGE_BYTES, Q_BYTES = HQ * D * 2;
static_assert(NL == 16 && PSTEP == 32 && NB == 512 && HQ == 2 * HKV, "layout below is written for these sizes");
constexpr int BLIND = 6;
constexpr int SNAP_AT = 1, NONCE_AT = 11, SOFT_AT = 6;
constexpr int NT = 2, SC1 = 16;
constexpr unsigned WS_IN = 0, WS_Q = HKV * 16 * NSPLIT * 128, WS_END = WS_Q + HKV * 16 * 4;
constexpr int CW = 2;
constexpr unsigned SPIN_CAP = 1u << 24, TAGM = 0x7fffu, ACK = 0x8000u;
constexpr float NEG = -3.0e38f;

using bf16x2 = __attribute__((ext_vector_type(2))) __bf16;
using u32x4 = __attribute__((ext_vector_type(4))) unsigned;
using i32x4 = __attribute__((ext_vector_type(4))) int;
using f32x4 = __attribute__((ext_vector_type(4))) float;
using f32x2 = __attribute__((ext_vector_type(2))) float;

__device__ __forceinline__ float dot2(unsigned k, unsigned q, float acc) {
  return __builtin_amdgcn_fdot2_f32_bf16(__builtin_bit_cast(bf16x2, k), __builtin_bit_cast(bf16x2, q), acc, false);
}
__device__ __forceinline__ float fmx(float a, float b) { return __builtin_elementwise_maximum(a, b); }
template <int CTRL> __device__ __forceinline__ float dpp(float x) {
  return __builtin_bit_cast(float, __builtin_amdgcn_mov_dpp(__builtin_bit_cast(int, x), CTRL, 0xf, 0xf, true));
}
constexpr int XOR1 = 0xB1, XOR2 = 0x4E, XOR7 = 0x141, XOR8 = 0x128, ROR4 = 0x124, BCAST = 0x150, EVEN = 0xA0, ODD = 0xF5, QUAD0 = 0x00, QUAD1 = 0x55;
__device__ __forceinline__ float xrow_max(float x) {
  auto s = __builtin_amdgcn_permlane16_swap(__float_as_uint(x), __float_as_uint(x), false, false);
  x = fmx(__uint_as_float(s[0]), __uint_as_float(s[1]));
  auto t = __builtin_amdgcn_permlane32_swap(__float_as_uint(x), __float_as_uint(x), false, false);
  return fmx(__uint_as_float(t[0]), __uint_as_float(t[1]));
}
__device__ __forceinline__ float xrow_sum(float x) {
  auto s = __builtin_amdgcn_permlane16_swap(__float_as_uint(x), __float_as_uint(x), false, false);
  x = __uint_as_float(s[0]) + __uint_as_float(s[1]);
  auto t = __builtin_amdgcn_permlane32_swap(__float_as_uint(x), __float_as_uint(x), false, false);
  return __uint_as_float(t[0]) + __uint_as_float(t[1]);
}
template <int N> struct IC { static constexpr int v = N; };
template <int I, int N, class F> __device__ __forceinline__ void sfor(F&& f) {
  if constexpr (I < N) { f(IC<I>{}); sfor<I + 1, N>(f); }
}
#define SB() __builtin_amdgcn_sched_barrier(0)
#define RSRC(p, bytes) __builtin_amdgcn_make_buffer_rsrc((void*)(p), 0, (bytes), 0x00020000)

__global__ void __launch_bounds__(WAVES * 64, WAVES / 4)
attn_decode(const unsigned short* __restrict__ kc, const unsigned short* __restrict__ vc, const unsigned short* __restrict__ q,
            const int* __restrict__ bt, const int* __restrict__ ctxp, unsigned char* __restrict__ ws, unsigned* __restrict__ out) {
  __shared__ __attribute__((aligned(16))) float lds_o[WAVES][2][D];
  __shared__ __attribute__((aligned(16))) float lds_ml[2][2][WAVES];
  __shared__ int lds_cnt[WAVES][16];
  const int b = blockIdx.x;
  const int h = (int)(0xecba8764320fd951ull >> (4 * (b >> 4))) & 15, s = b & 15;
  const int tid = threadIdx.x, lane = tid & 63, wave = __builtin_amdgcn_readfirstlane(tid >> 6), r = lane >> 4, c = lane & 15;
  const int q0 = s + NSPLIT * wave;
  int voff = (q0 * 4 + r) * TOK_BYTES + h * (D * 2) + c * 16;
  *(lane < 32 ? &lds_cnt[wave][c] : reinterpret_cast<int*>(&lds_ml[1][lane & 1][wave])) = lane < 32 ? 0 : -1;
  asm volatile("" : "+v"(voff));
  SB();
  __syncthreads();
  SB();
  asm volatile("" :: "s"(q), "s"(kc), "s"(vc), "s"(bt), "s"(ctxp), "s"(ws), "s"(out));
  typedef const __attribute__((address_space(4))) int* cint_p;
  const int ctx = ((cint_p)ctxp)[0];
  SB();
  auto rk = RSRC(kc, KV_BYTES);
  u32x4 X[2 * NL];
  sfor<0, WIN>([&](auto N) { X[N.v] = __builtin_amdgcn_raw_buffer_load_b128(rk, voff, N.v * PSTEP * PAGE_BYTES, NT); SB(); });
  u32x4 qv[2];
#pragma unroll
  for (int g = 0; g < 2; ++g) qv[g] = __builtin_amdgcn_raw_buffer_load_b128(RSRC(q, Q_BYTES), (2 * h + g) * (D * 2) + c * 16, 0, 0);
  SB();
  i32x4 e[2];
#pragma unroll
  for (int j = 0; j < 2; ++j) e[j] = __builtin_bit_cast(i32x4, __builtin_amdgcn_raw_buffer_load_b128(RSRC(bt, NB * 4), lane * 16 + j * 1024, 0, 0));
  SB();
  auto rv = RSRC(vc, KV_BYTES);
  auto rw = RSRC(ws, WS_END);
  const int qoff = WS_Q + (h * 16 + (wave == CW ? s : 8 * (wave & 1) + (lane >> 3))) * 4;
  const int in_o = WS_IN + (h * 16 + s) * (NSPLIT * 128) + lane * 16;
  unsigned nonce = 0, tq0, tq1;
  u32x4 olda, oldb;
  SB();
  const int len = ctx > 0 ? min(ctx, LK) : LK;
  const float c2 = ctx > 0 ? 0.088388347648318440f * 1.4426950408889634f : 0.f;
  const int nfull = len >> 4, rem = len & 15;
  float wgt[NG];
  unsigned live = 0;
  auto ld = [&](auto N) { constexpr int n = N.v;
    if constexpr (n < 2 * NL) {
      const unsigned so = (n < BLIND || ((live >> (2 * (n % NL))) & 1u)) ? (unsigned)((n % NL) * PSTEP * PAGE_BYTES) : KV_BYTES;
      X[n] = __builtin_amdgcn_raw_buffer_load_b128(n < NL ? rk : rv, voff, so, NT);
    } };
  SB();

  float sc[NG], x[16];
  sfor<0, NL>([&](auto N) {
    constexpr int n = N.v;
    asm volatile("" : "+v"(X[n]));
    SB();
    ld(IC<n + WIN>{});
    if constexpr (n == SNAP_AT) {
      tq0 = __builtin_amdgcn_raw_buffer_load_b32(rw, qoff, wave <= CW ? 0u : WS_END, SC1);
      olda = __builtin_amdgcn_raw_buffer_load_b128(rw, in_o, wave == CW ? 0u : WS_END, SC1);
      oldb = __builtin_amdgcn_raw_buffer_load_b128(rw, in_o + 1024, wave == CW ? 0u : WS_END, SC1);
    }
    SB();
    if constexpr (n == BLIND - WIN - 1) {
#pragma unroll
      for (int j = 0; j < 2; ++j)
#pragma unroll
        for (int k = 0; k < 4; ++k) {
          const int i = 256 * j + 4 * lane + k;
          const int inc = i < nfull ? 1 : (i == nfull ? 0x10000 : 0);
          if ((e[j][k] & (PSTEP - 1)) == (q0 >> 2)) atomicAdd(&lds_cnt[wave][(e[j][k] >> 5) & 15], inc);
        }
#pragma unroll
      for (int gi = 0; gi < NG; ++gi) {
        const int cw = lds_cnt[wave][8 * gi + (c >> 1)];
        const int cnt = (cw & 0xffff) + (4 * (q0 & 3) + r < rem ? cw >> 16 : 0);
        wgt[gi] = (float)cnt;
        live |= ((unsigned)__builtin_amdgcn_ballot_w64(cnt > 0) & 0xffffu) << (16 * gi);
      }
      SB();
    }
    if constexpr (n == NONCE_AT) {
      asm volatile("" :: "v"(olda), "v"(oldb));
      if (wave == CW) {
        nonce = __builtin_amdgcn_readfirstlane(tq0);
        for (;;) {
          ++nonce;
          const bool hit = !((olda[1] ^ nonce) & TAGM) || !((olda[3] ^ nonce) & TAGM) || !((oldb[1] ^ nonce) & TAGM) || !((oldb[3] ^ nonce) & TAGM);
          if (!__builtin_amdgcn_ballot_w64(hit)) break;
        }
      }
      __builtin_amdgcn_raw_buffer_store_b32(nonce, rw, lane == 0 ? qoff : (int)WS_END, wave == CW ? 0u : WS_END, SC1);
      SB();
    }
#pragma unroll
    for (int g = 0; g < 2; ++g) {
      float d = 0.f;
#pragma unroll
      for (int k = 0; k < 4; ++k) d = dot2(X[n][k], qv[g][k], d);
      asm volatile("" : "+v"(d));
      x[(n & 7) * 2 + g] = d;
    }
    if constexpr ((n & 7) == 7) {
#define TR_STEP(HALF, CTRL, BIT) _Pragma("unroll") for (int hh = 0; hh < (HALF); ++hh) { \
      const float lo_ = x[hh], hi_ = x[hh + (HALF)]; const float t1_ = lo_ + dpp<CTRL>(lo_), t2_ = hi_ + dpp<CTRL>(hi_); \
      x[hh] = (c & (BIT)) ? t2_ : t1_; }
      TR_STEP(8, XOR8, 8) TR_STEP(4, XOR7, 4) TR_STEP(2, XOR2, 2) TR_STEP(1, XOR1, 1)
#undef TR_STEP
      sc[n >> 3] = wgt[n >> 3] > 0.f ? x[0] * c2 : NEG;
    }
    SB();
  });
  float m = fmx(sc[0], sc[1]);
  m = fmx(m, dpp<XOR2>(m)); m = fmx(m, dpp<ROR4>(m)); m = fmx(m, dpp<XOR8>(m)); m = xrow_max(m);
  float p[NG];
#pragma unroll
  for (int gi = 0; gi < NG; ++gi) p[gi] = wgt[gi] * __builtin_amdgcn_exp2f(sc[gi] - m);
  float l = p[0] + p[1];
  l += dpp<XOR2>(l); l += dpp<ROR4>(l); l += dpp<XOR8>(l); l = xrow_sum(l);
  if (lane < 2) { lds_ml[0][lane][wave] = m; asm volatile("" ::: "memory"); lds_ml[1][lane][wave] = __builtin_fabsf(l); }

  f32x2 acc[2][4];
#pragma unroll
  for (int g = 0; g < 2; ++g)
#pragma unroll
    for (int k = 0; k < 4; ++k) acc[g][k] = f32x2{0.f, 0.f};
  float fs = 0.f;
  unsigned xw = 0, w1 = 0, w3 = 0;
  int oo = WS_IN + ((h * 16 + 8 * (wave & 1) + (lane >> 3)) * NSPLIT + s) * 128 + (lane & 7) * 16;
  typedef __attribute__((address_space(3))) f32x4* l4_p;
  typedef __attribute__((address_space(3))) const f32x2* l2_p;
  l4_p lo_w = (l4_p)&lds_o[wave][r >> 1][8 * c + 4 * (r & 1)];
  l2_p lo_r = (l2_p)&lds_o[0][wave & 1][2 * lane];
  int oout = lane < 8 ? ((2 * h + (s >> 3)) * (D / 2) + 8 * (s & 7) + lane) * 4 : (int)Q_BYTES;
  SB();
  sfor<0, NL>([&](auto N) {
    constexpr int n = N.v;
    asm volatile("" : "+v"(X[NL + n]));
    SB();
    if constexpr (n + WIN == NL - 2) tq1 = __builtin_amdgcn_raw_buffer_load_b32(rw, qoff, wave < 2 ? 0u : WS_END, SC1);
    ld(IC<NL + n + WIN>{});
    SB();
    const float p0 = dpp<BCAST + (n & 7) * 2 + 0>(p[n >> 3]);
    const float p1 = dpp<BCAST + (n & 7) * 2 + 1>(p[n >> 3]);
#pragma unroll
    for (int k = 0; k < 4; ++k) {
      const f32x2 vf = {__builtin_bit_cast(float, X[NL + n][k] << 16), __builtin_bit_cast(float, X[NL + n][k] & 0xffff0000u)};
      acc[0][k] = __builtin_elementwise_fma(f32x2{p0, p0}, vf, acc[0][k]);
      acc[1][k] = __builtin_elementwise_fma(f32x2{p1, p1}, vf, acc[1][k]);
    }
#pragma unroll
    for (int k = 0; k < 4; ++k) asm volatile("" : "+v"(acc[0][k]), "+v"(acc[1][k]));
    SB();
    if constexpr (n == SOFT_AT) {
      const int g = r >> 1;
      f32x4 ma, mb, la, lb;
      for (;;) {
        asm volatile("" ::: "memory");
        la = *reinterpret_cast<const f32x4*>(&lds_ml[1][g][0]); lb = *reinterpret_cast<const f32x4*>(&lds_ml[1][g][4]);
        asm volatile("" ::: "memory");
        ma = *reinterpret_cast<const f32x4*>(&lds_ml[0][g][0]); mb = *reinterpret_cast<const f32x4*>(&lds_ml[0][g][4]);
        const unsigned neg = (__float_as_uint(la[0]) | __float_as_uint(la[1]) | __float_as_uint(la[2]) | __float_as_uint(la[3])) |
                             (__float_as_uint(lb[0]) | __float_as_uint(lb[1]) | __float_as_uint(lb[2]) | __float_as_uint(lb[3]));
        if (!__builtin_amdgcn_ballot_w64((int)neg < 0)) break;
      }
      const float M = fmx(fmx(fmx(ma[0], ma[1]), fmx(ma[2], ma[3])), fmx(fmx(mb[0], mb[1]), fmx(mb[2], mb[3])));
      fs = __builtin_amdgcn_exp2f((g ? dpp<QUAD1>(m) : dpp<QUAD0>(m)) - M);
      float L = 0.f;
#pragma unroll
      for (int w = 0; w < 4; ++w) { L = fmaf(__builtin_amdgcn_exp2f(ma[w] - M), la[w], L); L = fmaf(__builtin_amdgcn_exp2f(mb[w] - M), lb[w], L); }
      const unsigned xh = __float_as_uint((lane & 1) ? L : M);
      auto t = __builtin_amdgcn_permlane32_swap(xh, xh, false, false);
      xw = (wave & 1) ? t[1] : t[0];
      asm volatile("" : "+v"(fs), "+v"(xw), "+v"(oo), "+v"(lo_w), "+v"(lo_r), "+v"(oout));
      SB();
    }
    if constexpr (n == NL - 2) {
      const unsigned tg = (tq1 & TAGM) | (tq1 == tq0 ? ACK : 0u);
      w1 = (xw & 0xffff0000u) | tg; w3 = (xw << 16) | tg;
      asm volatile("" : "+v"(w1), "+v"(w3));
      SB();
    }
  });

  float u[8];
#pragma unroll
  for (int k = 0; k < 8; ++k) {
    auto t = __builtin_amdgcn_permlane32_swap(__float_as_uint(acc[0][k >> 1][k & 1]), __float_as_uint(acc[1][k >> 1][k & 1]), false, false);
    u[k] = __uint_as_float(t[0]) + __uint_as_float(t[1]);
  }
  f32x4 o4;
#pragma unroll
  for (int k = 0; k < 4; ++k) {
    auto t = __builtin_amdgcn_permlane16_swap(__float_as_uint(u[k]), __float_as_uint(u[k + 4]), false, false);
    o4[k] = __uint_as_float(t[0]) + __uint_as_float(t[1]);
  }
  *lo_w = o4 * fs;
  __syncthreads();
  if (wave < 2) {
    f32x2 ow[WAVES];
#pragma unroll
    for (int w = 0; w < WAVES; ++w) ow[w] = lo_r[w * D];
    const f32x2 o = ((ow[0] + ow[1]) + (ow[2] + ow[3])) + ((ow[4] + ow[5]) + (ow[6] + ow[7]));
    __builtin_amdgcn_raw_buffer_store_b128(u32x4{__float_as_uint(o[0]), w1, __float_as_uint(o[1]), w3}, rw, oo, 0, SC1);
    for (unsigned spins = 0; __builtin_amdgcn_ballot_w64(tq1 == tq0) && spins < SPIN_CAP; ++spins) {
      __builtin_amdgcn_s_sleep(8);
      asm volatile("" ::: "memory");
      tq1 = __builtin_amdgcn_raw_buffer_load_b32(rw, qoff, 0, SC1);
      const unsigned tg = (tq1 & TAGM) | (tq1 == tq0 ? ACK : 0u);
      __builtin_amdgcn_raw_buffer_store_b128(u32x4{__float_as_uint(o[0]), (xw & 0xffff0000u) | tg, __float_as_uint(o[1]), (xw << 16) | tg}, rw, oo, 0, SC1);
    }
  } else if (wave == CW) {
    u32x4 ia, ib;
    for (unsigned spins = 0;; ++spins) {
      asm volatile("" ::: "memory");
      ia = __builtin_amdgcn_raw_buffer_load_b128(rw, in_o, 0, SC1);
      ib = __builtin_amdgcn_raw_buffer_load_b128(rw, in_o + 1024, 0, SC1);
      const unsigned miss = (((ia[1] ^ nonce) | (ia[3] ^ nonce)) | ((ib[1] ^ nonce) | (ib[3] ^ nonce))) & TAGM;
      if (!__builtin_amdgcn_ballot_w64(miss != 0) || spins >= SPIN_CAP) break;
    }
    const float xa = __uint_as_float((ia[1] & 0xffff0000u) | (ia[3] >> 16)), xb = __uint_as_float((ib[1] & 0xffff0000u) | (ib[3] >> 16));
    const float m_a = dpp<EVEN>(xa), m_b = dpp<EVEN>(xb), l_a = dpp<ODD>(xa), l_b = dpp<ODD>(xb);
    float M = fmx(m_a, m_b);
    M = fmx(M, dpp<XOR8>(M)); M = xrow_max(M);
    const float wa = __builtin_amdgcn_exp2f(m_a - M), wb = __builtin_amdgcn_exp2f(m_b - M);
    float n0 = wa * __uint_as_float(ia[0]) + wb * __uint_as_float(ib[0]);
    float n1 = wa * __uint_as_float(ia[2]) + wb * __uint_as_float(ib[2]);
    float dn = wa * l_a + wb * l_b;
    n0 += dpp<XOR8>(n0); n1 += dpp<XOR8>(n1); dn += dpp<XOR8>(dn);
    n0 = xrow_sum(n0); n1 = xrow_sum(n1); dn = xrow_sum(dn);
    const float inv = __builtin_amdgcn_rcpf(dn);
    const bf16x2 r2 = {(__bf16)(n0 * inv), (__bf16)(n1 * inv)};
    __builtin_amdgcn_raw_buffer_store_b32(__builtin_bit_cast(unsigned, r2), RSRC(out, Q_BYTES), oout, 0, SC1);
    if (__builtin_amdgcn_ballot_w64(((ia[1] | ia[3] | ib[1] | ib[3]) & ACK) != 0) && lane == 0)
      __builtin_amdgcn_raw_buffer_store_b32(nonce + 1, rw, qoff, 0, SC1);
  }
}

extern "C" void kernel_launch(void* const* d_in, const int* in_sizes, int n_in, void* d_out, int out_size, void* d_ws, size_t ws_size,
                              hipStream_t stream) {
  if (n_in != 5 || in_sizes[0] != HQ * D || in_sizes[1] != (int)(KV_BYTES / 2) || in_sizes[2] != (int)(KV_BYTES / 2) || in_sizes[3] != NB ||
      in_sizes[4] != 1 || out_size != HQ * D || ws_size < WS_END) return;
  attn_decode<<<HKV * NSPLIT, WAVES * 64, 0, stream>>>((const unsigned short*)d_in[1], (const unsigned short*)d_in[2], (const unsigned short*)d_in[0],
                                                        (const int*)d_in[3], (const int*)d_in[4], (unsigned char*)d_ws, (unsigned*)d_out);
}
```
